```python
import torch

def _softmax(x, axis=-1):
    return torch.softmax(x, dim=axis)
def _gelu(x):
    return torch.nn.functional.gelu(x, approximate="tanh")
def _silu(x):
    return torch.nn.functional.silu(x)
def _sigmoid(x):
    return torch.sigmoid(x)
def _layernorm(x, axis=-1, eps=1e-5):
    m = x.mean(dim=axis, keepdim=True)
    v = x.var(dim=axis, keepdim=True, unbiased=False)
    return (x - m) / torch.sqrt(v + eps)
def _scatter_add(tgt, idx, src, axis=0):
    return tgt.clone().index_add_(axis, idx, src)
def _segment_sum(src, seg, n):
    out = torch.zeros((n,) + src.shape[1:], dtype=src.dtype)
    return out.index_add_(0, seg, src)
def _pad(x, pw):
    flat = tuple(v for pair in reversed(pw) for v in pair)
    return torch.nn.functional.pad(x, flat)

kernel_name = 'standalone__softmax_full_bidir__decode'

def setup_inputs():

    _w = (lambda a: (lambda t: t.to(torch.bfloat16) if t.is_floating_point() else t)(torch.as_tensor(a)))
    B, H, Sq, Skv, Dh = 8, 32, 1, 4096, 128
    return {
        "q": _w((lambda *s: torch.randn(*s, dtype=torch.bfloat16) * 0.8)(B, H, Sq, Dh)),
        "k": _w((lambda *s: torch.randn(*s, dtype=torch.bfloat16) * 0.8)(B, H, Skv, Dh)),
        "v": _w((lambda *s: torch.randn(*s, dtype=torch.bfloat16) * 0.05)(B, H, Skv, Dh)),
    }

def reference(q, k, v):
    scale = q.shape[-1] ** -0.5
    scores = torch.einsum('bhqd,bhkd->bhqk', q, k) * scale
    attn = _softmax(scores, axis=-1)
    return torch.einsum('bhqk,bhkd->bhqd', attn, v)
```

```cpp
#include <hip/hip_runtime.h>
#include <hip/hip_bf16.h>
#include <stdint.h>
#include <float.h>

typedef unsigned u32x4 __attribute__((ext_vector_type(4)));
typedef __bf16 bf16x2 __attribute__((ext_vector_type(2)));
typedef float f32x4 __attribute__((ext_vector_type(4)));
typedef float f32x2 __attribute__((ext_vector_type(2)));
typedef unsigned long long u64x4 __attribute__((ext_vector_type(4)));
#define GAS __attribute__((address_space(1)))

constexpr int SKV = 4096, DH = 128;
constexpr int HEAD_BYTES = SKV * DH * 2;
constexpr int NW = 2;
constexpr int D = 2;
constexpr int TLD = 8;
constexpr int TB = TLD * 1024, STEP = TB * NW;
constexpr int NT = HEAD_BYTES / STEP;
constexpr int G = NT / D;
#define PACE_TAU16 892
#define PACE_START16 1280
#define PACE_HOLD_K 9
#define PACE_HOLD_V 6
constexpr float C2 = 0.088388347648318440f * 1.4426950408889634f;

__device__ __forceinline__ float dot2(unsigned k, unsigned q, float acc) {
  return __builtin_amdgcn_fdot2_f32_bf16(__builtin_bit_cast(bf16x2, k), __builtin_bit_cast(bf16x2, q), acc, false);
}
template <int CTRL> __device__ __forceinline__ float dpp(float x) {
  return __builtin_bit_cast(float, __builtin_amdgcn_mov_dpp(__builtin_bit_cast(int, x), CTRL, 0xf, 0xf, true));
}
constexpr int ROR8 = 0x128, ROR4 = 0x124, ROR2 = 0x122, ROR1 = 0x121;
__device__ __forceinline__ float xrow_max(float x) {
  auto s = __builtin_amdgcn_permlane16_swap(__float_as_uint(x), __float_as_uint(x), false, false);
  x = fmaxf(__uint_as_float(s[0]), __uint_as_float(s[1]));
  auto t = __builtin_amdgcn_permlane32_swap(__float_as_uint(x), __float_as_uint(x), false, false);
  return fmaxf(__uint_as_float(t[0]), __uint_as_float(t[1]));
}
__device__ __forceinline__ float xrow_sum(float x) {
  auto s = __builtin_amdgcn_permlane16_swap(__float_as_uint(x), __float_as_uint(x), false, false);
  x = __uint_as_float(s[0]) + __uint_as_float(s[1]);
  auto t = __builtin_amdgcn_permlane32_swap(__float_as_uint(x), __float_as_uint(x), false, false);
  return __uint_as_float(t[0]) + __uint_as_float(t[1]);
}

__global__ void __launch_bounds__(NW * 64)
attn_decode_kernel(const unsigned char* q_arg, const unsigned char* k_arg, const unsigned char* v_arg, unsigned* out_arg) {
  __shared__ __attribute__((aligned(16))) float lds[SKV + 16 + NW * 4 * DH];
  unsigned long long t0; u64x4 ka;
  asm volatile("s_memrealtime %0\n\ts_load_dwordx8 %1, %2, 0x0\n\ts_waitcnt lgkmcnt(0)"
               : "=&s"(t0), "=&s"(ka) : "s"(__builtin_amdgcn_kernarg_segment_ptr()) : "memory");
  (void)q_arg; (void)k_arg; (void)v_arg; (void)out_arg;
  const GAS unsigned char* q = (const GAS unsigned char*)ka[0];
  const unsigned char* k = (const unsigned char*)ka[1];
  const unsigned char* v = (const unsigned char*)ka[2];
  GAS unsigned* out = (GAS unsigned*)ka[3];
  unsigned sched = (unsigned)t0 * 16u + PACE_START16;
  unsigned tnow = (unsigned)t0;
#define HOLD(n) do { if ((int)(tnow * 16u - sched) < 0) __builtin_amdgcn_s_sleep(n); sched += PACE_TAU16; \
    __builtin_amdgcn_sched_barrier(0); } while (0)
#define SAMPLE() do { tnow = (unsigned)__builtin_amdgcn_s_memrealtime(); __builtin_amdgcn_sched_barrier(0); } while (0)
  const int b = blockIdx.x;
  const int bh = (b & 7) * 32 + (b >> 3);
  const int tid = threadIdx.x, lane = tid & 63;
  const int wave = __builtin_amdgcn_readfirstlane(tid >> 6);
  const int c = lane & 15, r = lane >> 4;
  auto rk = __builtin_amdgcn_make_buffer_rsrc((void*)(k + (size_t)bh * HEAD_BYTES), 0, HEAD_BYTES, 0x00020000);
  auto rv = __builtin_amdgcn_make_buffer_rsrc((void*)(v + (size_t)bh * HEAD_BYTES), 0, HEAD_BYTES, 0x00020000);
  const int voff = lane * 16;
  const unsigned wrot = (unsigned)wave * TB + (unsigned)(b >> 3) * STEP;
#define TOFF(g, d) ((wrot + (unsigned)(g) * (D * STEP) + (d) * STEP) & (HEAD_BYTES - 1))
#define ISSUE(d, rs, so) do { const unsigned so_ = (so); \
    _Pragma("unroll") for (int t_ = 0; t_ < TLD; ++t_) { \
      R[d][t_] = __builtin_amdgcn_raw_buffer_load_b128(rs, voff + (t_ & 3) * 1024, so_ + (t_ >> 2) * 4096, 2  ); \
      __builtin_amdgcn_sched_barrier(0); } } while (0)
  u32x4 R[D][TLD];
  const u32x4 qv = *(const GAS u32x4*)(q + (size_t)bh * (DH * 2) + c * 16);
  __builtin_amdgcn_sched_barrier(0);
#pragma unroll
  for (int d = 0; d < D; ++d) { ISSUE(d, rk, TOFF(0, d)); }
  float* sw = lds + wave * (NT * 4 * TLD);
  float m = -FLT_MAX;
  for (int g = 0; g < G; ++g) {
    const bool last = g + 1 == G;
    auto rn = last ? rv : rk;
    const int gn = last ? 0 : g + 1;
#pragma unroll
    for (int d = 0; d < D; ++d) {
      float s[TLD];
#pragma unroll
      for (int t = 0; t < TLD; ++t) {
        float x = dot2(R[d][t][0], qv[0], 0.f);
        x = dot2(R[d][t][1], qv[1], x);
        x = dot2(R[d][t][2], qv[2], x);
        s[t] = dot2(R[d][t][3], qv[3], x);
      }
#pragma unroll
      for (int t = 0; t < TLD; ++t) s[t] += dpp<ROR8>(s[t]);
#pragma unroll
      for (int t = 0; t < TLD; ++t) s[t] += dpp<ROR4>(s[t]);
#pragma unroll
      for (int t = 0; t < TLD; ++t) s[t] += dpp<ROR2>(s[t]);
#pragma unroll
      for (int t = 0; t < TLD; ++t) s[t] += dpp<ROR1>(s[t]);
#pragma unroll
      for (int t = 0; t < TLD; ++t) { m = fmaxf(m, s[t]); asm volatile("" : "+v"(s[t])); }
      if (c == 0) {
        float* p = sw + ((g * D + d) * 4 + r) * TLD;
#pragma unroll
        for (int t = 0; t < TLD; t += 4) *reinterpret_cast<f32x4*>(p + t) = f32x4{s[t], s[t + 1], s[t + 2], s[t + 3]};
      }
      __builtin_amdgcn_sched_barrier(0);
      HOLD(PACE_HOLD_K);
      ISSUE(d, rn, TOFF(gn, d));
      SAMPLE();
    }
  }
  const float M = xrow_max(m);
  float l = 0.f;
  f32x2 acc[4] = {{0.f, 0.f}, {0.f, 0.f}, {0.f, 0.f}, {0.f, 0.f}};
  float p[TLD];
#define GETP(tile) do { const float* pp_ = sw + ((tile) * 4 + r) * TLD; \
    _Pragma("unroll") for (int h_ = 0; h_ < TLD / 4; ++h_) { const f32x4 x_ = *reinterpret_cast<const f32x4*>(pp_ + 4 * h_); \
      _Pragma("unroll") for (int j_ = 0; j_ < 4; ++j_) { p[4 * h_ + j_] = __builtin_amdgcn_exp2f((x_[j_] - M) * C2); l += p[4 * h_ + j_]; } } \
    _Pragma("unroll") for (int t_ = 0; t_ < TLD; ++t_) asm volatile("" : "+v"(p[t_])); \
    asm volatile("" : "+v"(l)); __builtin_amdgcn_sched_barrier(0); } while (0)
#define PV(d) do { _Pragma("unroll") for (int t_ = 0; t_ < TLD; ++t_) { const f32x2 p2_ = {p[t_], p[t_]}; \
      _Pragma("unroll") for (int kk_ = 0; kk_ < 4; ++kk_) { \
        const f32x2 vv_ = {__builtin_bit_cast(float, R[d][t_][kk_] << 16), __builtin_bit_cast(float, R[d][t_][kk_] & 0xffff0000u)}; \
        acc[kk_] = __builtin_elementwise_fma(p2_, vv_, acc[kk_]); } } \
      asm volatile("" : "+v"(acc[0]), "+v"(acc[1]), "+v"(acc[2]), "+v"(acc[3]) :: "memory"); \
      __builtin_amdgcn_sched_barrier(0); } while (0)
  GETP(0);
  for (int g = 0; g < G - 1; ++g) {
#pragma unroll
    for (int d = 0; d < D; ++d) {
      PV(d);
      HOLD(PACE_HOLD_V);
      ISSUE(d, rv, TOFF(g + 1, d));
      SAMPLE();
      GETP(g * D + d + 1);
    }
  }
#pragma unroll
  for (int d = 0; d < D - 1; ++d) { PV(d); GETP((G - 1) * D + d + 1); }
  {
    const float L = xrow_sum(l);
    if (lane == 0) *reinterpret_cast<float2*>(lds + SKV + 2 * wave) = make_float2(M, L);
    __builtin_amdgcn_sched_barrier(0);
  }
  PV(D - 1);
#undef GETP
#undef PV
#undef ISSUE
#undef TOFF
#undef HOLD
#undef SAMPLE
  float* pa = lds + SKV + 16 + (wave * 4 + r) * DH + 8 * c;
  *reinterpret_cast<f32x4*>(pa) = f32x4{acc[0][0], acc[0][1], acc[1][0], acc[1][1]};
  *reinterpret_cast<f32x4*>(pa + 4) = f32x4{acc[2][0], acc[2][1], acc[3][0], acc[3][1]};
  __syncthreads();
  if (wave == 0) {
    float Mw[NW], Lw[NW];
#pragma unroll
    for (int i = 0; i < NW; ++i) { const float2 x = *reinterpret_cast<const float2*>(lds + SKV + 2 * i); Mw[i] = x.x; Lw[i] = x.y; }
    f32x2 sum[NW];
#pragma unroll
    for (int i = 0; i < NW; ++i) {
      const float* b0 = lds + SKV + 16 + i * 4 * DH + 2 * lane;
      const f32x2 x0 = *reinterpret_cast<const f32x2*>(b0), x1 = *reinterpret_cast<const f32x2*>(b0 + DH);
      const f32x2 x2 = *reinterpret_cast<const f32x2*>(b0 + 2 * DH), x3 = *reinterpret_cast<const f32x2*>(b0 + 3 * DH);
      sum[i] = (x0 + x1) + (x2 + x3);
    }
    float MM = Mw[0];
#pragma unroll
    for (int i = 1; i < NW; ++i) MM = fmaxf(MM, Mw[i]);
    float LL = 0.f; f32x2 o2 = {0.f, 0.f};
#pragma unroll
    for (int i = 0; i < NW; ++i) {
      const float wt = __builtin_amdgcn_exp2f((Mw[i] - MM) * C2);
      LL = fmaf(wt, Lw[i], LL);
      o2 = __builtin_elementwise_fma(f32x2{wt, wt}, sum[i], o2);
    }
    const float inv = __builtin_amdgcn_rcpf(LL);
    bf16x2 o; o[0] = (__bf16)(o2[0] * inv); o[1] = (__bf16)(o2[1] * inv);
    out[bh * (DH / 2) + lane] = __builtin_bit_cast(unsigned, o);
  }
}

extern "C" void kernel_launch(void* const* d_in, const int* in_sizes, int n_in,
                              void* d_out, int out_size, void* d_ws, size_t ws_size,
                              hipStream_t stream) {
  (void)in_sizes; (void)n_in; (void)d_ws; (void)ws_size;
  const int nheads = out_size / DH;
  attn_decode_kernel<<<nheads, NW * 64, 0, stream>>>((const unsigned char*)d_in[0], (const unsigned char*)d_in[1],
                                                      (const unsigned char*)d_in[2], (unsigned*)d_out);
}
```
